```python
import torch

def _softmax(x, axis=-1):
    return torch.softmax(x, dim=axis)
def _gelu(x):
    return torch.nn.functional.gelu(x, approximate="tanh")
def _silu(x):
    return torch.nn.functional.silu(x)
def _sigmoid(x):
    return torch.sigmoid(x)
def _layernorm(x, axis=-1, eps=1e-5):
    m = x.mean(dim=axis, keepdim=True)
    v = x.var(dim=axis, keepdim=True, unbiased=False)
    return (x - m) / torch.sqrt(v + eps)
def _scatter_add(tgt, idx, src, axis=0):
    return tgt.clone().index_add_(axis, idx, src)
def _segment_sum(src, seg, n):
    out = torch.zeros((n,) + src.shape[1:], dtype=src.dtype)
    return out.index_add_(0, seg, src)
def _pad(x, pw):
    flat = tuple(v for pair in reversed(pw) for v in pair)
    return torch.nn.functional.pad(x, flat)

kernel_name = 'concat_heads__gqa_block_cached__gqa_block_cached__decode'

def setup_inputs():

    _w = (lambda a: (lambda t: t.to(torch.bfloat16) if t.is_floating_point() else t)(torch.as_tensor(a)))
    def _A_setup():
        B, Sq, D = 8, 1, 512
        Hq, Hkv, Sk, Dh = 32, 8, 8192, 128
        assert Hq % Hkv == 0, (Hq, Hkv)
        return {
            "x": _w((lambda *s: torch.randn(*s, dtype=torch.bfloat16) * 0.8)(B, Sq, D)),
            "k_cache": _w((lambda *s: torch.randn(*s, dtype=torch.bfloat16) * 0.8)(B, Hkv, Sk, Dh)),
            "v_cache": _w((lambda *s: torch.randn(*s, dtype=torch.bfloat16) * 0.05)(B, Hkv, Sk, Dh)),
            "wq": _w((lambda *s: torch.randn(*s, dtype=torch.bfloat16) * 0.8)(D, Hq * Dh) * (D ** -0.5)),
            "wo": _w((lambda *s: torch.randn(*s, dtype=torch.bfloat16) * 0.05)(Hq * Dh, D) * ((Hq * Dh) ** -0.5)),
        }
    _A_in = _A_setup()

    def _B_setup():
        B, Sq, D = 8, 1, 512
        Hq, Hkv, Sk, Dh = 32, 8, 8192, 128
        assert Hq % Hkv == 0, (Hq, Hkv)
        return {
            "x": _w((lambda *s: torch.randn(*s, dtype=torch.bfloat16) * 0.8)(B, Sq, D)),
            "k_cache": _w((lambda *s: torch.randn(*s, dtype=torch.bfloat16) * 0.8)(B, Hkv, Sk, Dh)),
            "v_cache": _w((lambda *s: torch.randn(*s, dtype=torch.bfloat16) * 0.05)(B, Hkv, Sk, Dh)),
            "wq": _w((lambda *s: torch.randn(*s, dtype=torch.bfloat16) * 0.8)(D, Hq * Dh) * (D ** -0.5)),
            "wo": _w((lambda *s: torch.randn(*s, dtype=torch.bfloat16) * 0.05)(Hq * Dh, D) * ((Hq * Dh) ** -0.5)),
        }
    _B_in = _B_setup()

    return {
        "A_x": _A_in["x"],
        "A_k_cache": _A_in["k_cache"],
        "A_v_cache": _A_in["v_cache"],
        "A_wq": _A_in["wq"],
        "A_wo": _A_in["wo"],
        "B_x": _B_in["x"],
        "B_k_cache": _B_in["k_cache"],
        "B_v_cache": _B_in["v_cache"],
        "B_wq": _B_in["wq"],
        "B_wo": _B_in["wo"],
    }

def reference(A_x, A_k_cache, A_v_cache, A_wq, A_wo, B_x, B_k_cache, B_v_cache, B_wq, B_wo):
    def _A_ref(x, k_cache, v_cache, wq, wo):
        B, Sq, D = x.shape
        Hq, Dh = 32, 128
        Hkv = k_cache.shape[1]
        rep = Hq // Hkv
        q = (lambda x, *d: x.permute(*d))((x @ wq).reshape(B, Sq, Hq, Dh), 0, 2, 1, 3)
        k = (lambda x, n, axis: torch.repeat_interleave(x, n, dim=axis))(k_cache, rep, axis=1)
        v = (lambda x, n, axis: torch.repeat_interleave(x, n, dim=axis))(v_cache, rep, axis=1)
        scores = torch.einsum("bhqd,bhkd->bhqk", q, k) * (Dh ** -0.5)
        p = _softmax(scores, axis=-1)
        o = torch.einsum("bhqk,bhkd->bhqd", p, v)
        return (lambda x, *d: x.permute(*d))(o, 0, 2, 1, 3).reshape(B, Sq, Hq * Dh) @ wo

    def _B_ref(x, k_cache, v_cache, wq, wo):
        B, Sq, D = x.shape
        Hq, Dh = 32, 128
        Hkv = k_cache.shape[1]
        rep = Hq // Hkv
        q = (lambda x, *d: x.permute(*d))((x @ wq).reshape(B, Sq, Hq, Dh), 0, 2, 1, 3)
        k = (lambda x, n, axis: torch.repeat_interleave(x, n, dim=axis))(k_cache, rep, axis=1)
        v = (lambda x, n, axis: torch.repeat_interleave(x, n, dim=axis))(v_cache, rep, axis=1)
        scores = torch.einsum("bhqd,bhkd->bhqk", q, k) * (Dh ** -0.5)
        p = _softmax(scores, axis=-1)
        o = torch.einsum("bhqk,bhkd->bhqd", p, v)
        return (lambda x, *d: x.permute(*d))(o, 0, 2, 1, 3).reshape(B, Sq, Hq * Dh) @ wo

    a = _A_ref(A_x, A_k_cache, A_v_cache, A_wq, A_wo)
    b = _B_ref(B_x, B_k_cache, B_v_cache, B_wq, B_wo)
    return torch.concatenate([a, b], axis=-1)
```

```cpp
#include <hip/hip_runtime.h>
#include <hip/hip_bf16.h>
#include <stdint.h>

#define LAS __attribute__((address_space(3)))
typedef __attribute__((ext_vector_type(8))) short bf16x8;
typedef __attribute__((ext_vector_type(4))) short bf16x4;
typedef __attribute__((ext_vector_type(4))) float f32x4;
typedef __attribute__((ext_vector_type(4))) unsigned u32x4;
typedef __attribute__((ext_vector_type(2))) unsigned u32x2;

constexpr int NB = 8, DM = 512, HQ = 32, HKV = 8, SK = 8192, DH = 128;
constexpr int NWG = 256, WAVES = 8, NTHR = 512;
constexpr int TOK_PER_WG = SK / 2;
constexpr int TILE = 32;
constexpr int TILES_PER_WAVE = TOK_PER_WG / TILE / WAVES;
constexpr float SCALE_LOG2E = 0.08838834764831845f * 1.4426950408889634f;

constexpr size_t WS_Q = 0;
constexpr size_t WS_PART = 131072;
constexpr size_t WS_ML = WS_PART + 256 * 512 * 4;
constexpr size_t WS_P2 = WS_ML + 256 * 8 * 4;

typedef __attribute__((ext_vector_type(2))) __bf16 bf16x2_t;
typedef __attribute__((ext_vector_type(2))) float f32x2;
__device__ __forceinline__ unsigned cvt_pk_bf16(float lo, float hi) {
  f32x2 v = {lo, hi};
  return __builtin_bit_cast(unsigned, __builtin_convertvector(v, bf16x2_t));
}
#define MFMA16(a, b, c) __builtin_amdgcn_mfma_f32_16x16x32_bf16((a), (b), (c), 0, 0, 0)

__device__ __forceinline__ void dma4(unsigned lds_dst, const void* sbase, unsigned v0, unsigned v1, unsigned v2, unsigned v3) {
  unsigned keep;
  asm volatile("s_mov_b32 %0, m0\n\ts_mov_b32 m0, %1\n\ts_nop 3\n\t"
               "global_load_lds_dwordx4 %3, %2 offset:0 nt\n\t"
               "global_load_lds_dwordx4 %4, %2 offset:1024 nt\n\t"
               "global_load_lds_dwordx4 %5, %2 offset:2048 nt\n\t"
               "global_load_lds_dwordx4 %6, %2 offset:3072 nt\n\t"
               "s_mov_b32 m0, %0"
               : "=&s"(keep) : "s"(lds_dst), "s"(sbase), "v"(v0), "v"(v1), "v"(v2), "v"(v3) : "memory");
}

__device__ __forceinline__ float xg_max(float x) {
  auto s = __builtin_amdgcn_permlane16_swap(__float_as_uint(x), __float_as_uint(x), false, false);
  x = fmaxf(__uint_as_float(s[0]), __uint_as_float(s[1]));
  auto t = __builtin_amdgcn_permlane32_swap(__float_as_uint(x), __float_as_uint(x), false, false);
  return fmaxf(__uint_as_float(t[0]), __uint_as_float(t[1]));
}
__device__ __forceinline__ float xg_sum(float x) {
  auto s = __builtin_amdgcn_permlane16_swap(__float_as_uint(x), __float_as_uint(x), false, false);
  x = __uint_as_float(s[0]) + __uint_as_float(s[1]);
  auto t = __builtin_amdgcn_permlane32_swap(__float_as_uint(x), __float_as_uint(x), false, false);
  return __uint_as_float(t[0]) + __uint_as_float(t[1]);
}

struct Ptrs {
  const uint16_t* x[2]; const uint16_t* k[2]; const uint16_t* v[2]; const uint16_t* wq[2]; const uint16_t* wo[2];
  uint16_t* out; unsigned char* ws;
};

__global__ void __launch_bounds__(NTHR) k_qproj(Ptrs P) {
  extern __shared__ __attribute__((aligned(16))) unsigned char smem[];
  const int tid = threadIdx.x, lane = tid & 63, wave = __builtin_amdgcn_readfirstlane(tid >> 6);
  const int br = blockIdx.x >> 7, cs = blockIdx.x & 127;
  const int g = lane >> 4, r16 = lane & 15, q4 = (lane & 15) >> 2, p4 = lane & 3;
  const uint16_t* wq = P.wq[br]; const uint16_t* x = P.x[br];
  const unsigned lds0 = (unsigned)(size_t)(LAS unsigned char*)smem;
  {
    const unsigned char* sb = (const unsigned char*)wq + (size_t)(64 * wave) * 8192 + cs * 64;
    unsigned vo[4];
#pragma unroll
    for (int p = 0; p < 4; ++p) vo[p] = (unsigned)((16 * p + (lane >> 2)) * 8192 + (lane & 3) * 16 - 1024 * p);
    dma4(lds0 + wave * 4096, sb, vo[0], vo[1], vo[2], vo[3]);
  }
  bf16x8 xb[2];
#pragma unroll
  for (int s = 0; s < 2; ++s) xb[s] = *(const bf16x8*)(x + (r16 & 7) * DM + 64 * wave + 32 * s + 8 * g);
  asm volatile("s_waitcnt vmcnt(0)" ::: "memory");
  f32x4 acc[2] = {{0, 0, 0, 0}, {0, 0, 0, 0}};
#pragma unroll
  for (int s = 0; s < 2; ++s)
#pragma unroll
    for (int cb = 0; cb < 2; ++cb) {
      const unsigned a0 = lds0 + wave * 4096 + (32 * s + 8 * g + q4) * 64 + cb * 32 + p4 * 8;
      bf16x4 lo, hi;
      asm volatile("ds_read_b64_tr_b16 %0, %2\n\tds_read_b64_tr_b16 %1, %2 offset:256\n\ts_waitcnt lgkmcnt(0)" : "=&v"(lo), "=&v"(hi) : "v"(a0) : "memory");
      bf16x8 a = __builtin_shufflevector(lo, hi, 0, 1, 2, 3, 4, 5, 6, 7);
      acc[cb] = MFMA16(a, xb[s], acc[cb]);
    }
  __syncthreads();
  float* red = (float*)smem;
#pragma unroll
  for (int cb = 0; cb < 2; ++cb)
#pragma unroll
    for (int j = 0; j < 4; ++j) red[((wave * 2 + cb) * 4 + j) * 64 + lane] = acc[cb][j];
  __syncthreads();
  {
    const int cb = tid >> 8, j = (tid >> 6) & 3;
    float s = 0.f;
#pragma unroll
    for (int w = 0; w < 8; ++w) s += red[((w * 2 + cb) * 4 + j) * 64 + lane];
    if (r16 < 8) {
      uint16_t* qo = (uint16_t*)(P.ws + WS_Q);
      const int col = 32 * cs + 16 * cb + 4 * g + j;
      qo[(br * NB + r16) * 4096 + col] = (uint16_t)(cvt_pk_bf16(s, 0.f) & 0xffff);
    }
  }
}

__global__ void __launch_bounds__(NTHR) k_attn(Ptrs P) {
  extern __shared__ __attribute__((aligned(16))) unsigned char smem[];
  const int tid = threadIdx.x, lane = tid & 63, wave = __builtin_amdgcn_readfirstlane(tid >> 6);
  const int wg = blockIdx.x;
  const int br = wg >> 7, seq = (wg >> 1) & 63, half = wg & 1;
  const int b = seq >> 3, kvh = seq & 7;
  const int g = lane >> 4, r16 = lane & 15, q4 = r16 >> 2, p4 = lane & 3;
  const unsigned lds0 = (unsigned)(size_t)(LAS unsigned char*)smem;
  const unsigned slotK = lds0 + wave * 16384, slotV = slotK + 8192;
  const unsigned char* kbase = (const unsigned char*)P.k[br] + ((size_t)seq * SK + (size_t)half * TOK_PER_WG) * 256 + (size_t)wave * 8192;
  const unsigned char* vbase = (const unsigned char*)P.v[br] + ((size_t)seq * SK + (size_t)half * TOK_PER_WG) * 256 + (size_t)wave * 8192;
  unsigned vk[4], vv[2];
#pragma unroll
  for (int p = 0; p < 4; ++p) vk[p] = (unsigned)(g * 256 + ((r16 ^ ((4 * p + g) & 15)) * 16));
#pragma unroll
  for (int p = 0; p < 2; ++p) vv[p] = (unsigned)(g * 256 + ((r16 ^ (((4 * p + g) & 7) << 1)) * 16));
  auto issueK = [&](int i) { const unsigned char* s = kbase + (size_t)i * (8192 * WAVES);
    dma4(slotK, s, vk[0], vk[1], vk[2], vk[3]); dma4(slotK + 4096, s + 4096, vk[0], vk[1], vk[2], vk[3]); };
  auto issueV = [&](int i) { const unsigned char* s = vbase + (size_t)i * (8192 * WAVES);
    dma4(slotV, s, vv[0], vv[1], vv[0], vv[1]); dma4(slotV + 4096, s + 4096, vv[0], vv[1], vv[0], vv[1]); };
  bf16x8 qf[4];
  {
    const uint16_t* qp = (const uint16_t*)(P.ws + WS_Q) + (br * NB + b) * 4096 + (kvh * 4 + (r16 & 3)) * DH + 8 * g;
#pragma unroll
    for (int s = 0; s < 4; ++s) { bf16x8 t = *(const bf16x8*)(qp + 32 * s); if (r16 >= 4) t = bf16x8{0, 0, 0, 0, 0, 0, 0, 0}; qf[s] = t; }
  }
  asm volatile("" : "+v"(qf[0]), "+v"(qf[1]), "+v"(qf[2]), "+v"(qf[3]));
  issueK(0); issueV(0);
  unsigned ak[4], av0;
#pragma unroll
  for (int s = 0; s < 4; ++s) ak[s] = slotK + r16 * 256 + (((4 * s + g) ^ r16) * 16);
  av0 = slotV + (4 * g + q4) * 256 + (((4 * g + q4) & 7) << 5) + p4 * 8;
  float m = -INFINITY, l = 0.f;
  f32x4 o[8];
#pragma unroll
  for (int i = 0; i < 8; ++i) o[i] = f32x4{0, 0, 0, 0};

  for (int it = 0; it < TILES_PER_WAVE; ++it) {
    const bool more = it + 1 < TILES_PER_WAVE;
    asm volatile("s_waitcnt vmcnt(8)" ::: "memory");
    bf16x8 kf[8];
    asm volatile("ds_read_b128 %0, %8\n\tds_read_b128 %1, %9\n\tds_read_b128 %2, %10\n\tds_read_b128 %3, %11\n\t"
                 "ds_read_b128 %4, %8 offset:4096\n\tds_read_b128 %5, %9 offset:4096\n\tds_read_b128 %6, %10 offset:4096\n\tds_read_b128 %7, %11 offset:4096\n\t"
                 "s_waitcnt lgkmcnt(0)"
                 : "=&v"(kf[0]), "=&v"(kf[1]), "=&v"(kf[2]), "=&v"(kf[3]), "=&v"(kf[4]), "=&v"(kf[5]), "=&v"(kf[6]), "=&v"(kf[7])
                 : "v"(ak[0]), "v"(ak[1]), "v"(ak[2]), "v"(ak[3]) : "memory");
    if (more) { issueK(it + 1); asm volatile("s_waitcnt vmcnt(8)" ::: "memory"); }
    else asm volatile("s_waitcnt vmcnt(0)" ::: "memory");
    bf16x4 vlo[8], vhi[8];
#pragma unroll
    for (int hb = 0; hb < 2; ++hb) {
      const unsigned a0 = av0 ^ ((4 * hb + 0) << 5), a1 = av0 ^ ((4 * hb + 1) << 5), a2 = av0 ^ ((4 * hb + 2) << 5), a3 = av0 ^ ((4 * hb + 3) << 5);
      asm volatile("ds_read_b64_tr_b16 %0, %8\n\tds_read_b64_tr_b16 %1, %9\n\tds_read_b64_tr_b16 %2, %10\n\tds_read_b64_tr_b16 %3, %11\n\t"
                   "ds_read_b64_tr_b16 %4, %8 offset:4096\n\tds_read_b64_tr_b16 %5, %9 offset:4096\n\tds_read_b64_tr_b16 %6, %10 offset:4096\n\tds_read_b64_tr_b16 %7, %11 offset:4096\n\t"
                   "s_waitcnt lgkmcnt(0)"
                   : "=&v"(vlo[4 * hb + 0]), "=&v"(vlo[4 * hb + 1]), "=&v"(vlo[4 * hb + 2]), "=&v"(vlo[4 * hb + 3]),
                     "=&v"(vhi[4 * hb + 0]), "=&v"(vhi[4 * hb + 1]), "=&v"(vhi[4 * hb + 2]), "=&v"(vhi[4 * hb + 3])
                   : "v"(a0), "v"(a1), "v"(a2), "v"(a3) : "memory");
    }
    if (more) issueV(it + 1);
    f32x4 s0 = {0, 0, 0, 0}, s1 = {0, 0, 0, 0};
#pragma unroll
    for (int s = 0; s < 4; ++s) { s0 = MFMA16(kf[s], qf[s], s0); s1 = MFMA16(kf[4 + s], qf[s], s1); }
    float mx = fmaxf(fmaxf(fmaxf(s0[0], s0[1]), fmaxf(s0[2], s0[3])), fmaxf(fmaxf(s1[0], s1[1]), fmaxf(s1[2], s1[3])));
    mx = xg_max(mx);
    const float mn = fmaxf(m, mx);
    const float alpha = __builtin_amdgcn_exp2f((m - mn) * SCALE_LOG2E);
    const float mc = mn * SCALE_LOG2E;
    float p[8];
#pragma unroll
    for (int j = 0; j < 4; ++j) { p[j] = __builtin_amdgcn_exp2f(fmaf(s0[j], SCALE_LOG2E, -mc)); p[4 + j] = __builtin_amdgcn_exp2f(fmaf(s1[j], SCALE_LOG2E, -mc)); }
    l = l * alpha + (((p[0] + p[1]) + (p[2] + p[3])) + ((p[4] + p[5]) + (p[6] + p[7])));
    m = mn;
    u32x4 pk = {cvt_pk_bf16(p[0], p[1]), cvt_pk_bf16(p[2], p[3]), cvt_pk_bf16(p[4], p[5]), cvt_pk_bf16(p[6], p[7])};
    const bf16x8 pb = __builtin_bit_cast(bf16x8, pk);
#pragma unroll
    for (int blk = 0; blk < 8; ++blk) {
      o[blk] *= alpha;
      const bf16x8 a = __builtin_shufflevector(vlo[blk], vhi[blk], 0, 1, 2, 3, 4, 5, 6, 7);
      o[blk] = MFMA16(a, pb, o[blk]);
    }
  }
  l = xg_sum(l);
  __syncthreads();
  float* mo = (float*)smem;
  float* mml = mo + 8 * 512;
  if (r16 < 4) {
#pragma unroll
    for (int blk = 0; blk < 8; ++blk) *(f32x4*)(mo + (wave * 4 + r16) * 128 + 16 * blk + 4 * g) = o[blk];
    if (g == 0) { mml[(wave * 4 + r16) * 2] = m; mml[(wave * 4 + r16) * 2 + 1] = l; }
  }
  __syncthreads();
  {
    const int h = tid >> 7, d = tid & 127;
    float M = -INFINITY;
#pragma unroll
    for (int w = 0; w < 8; ++w) M = fmaxf(M, mml[(w * 4 + h) * 2]);
    float L = 0.f, acc = 0.f;
#pragma unroll
    for (int w = 0; w < 8; ++w) {
      const float wt = __builtin_amdgcn_exp2f((mml[(w * 4 + h) * 2] - M) * SCALE_LOG2E);
      L = fmaf(wt, mml[(w * 4 + h) * 2 + 1], L);
      acc = fmaf(wt, mo[(w * 4 + h) * 128 + d], acc);
    }
    float* part = (float*)(P.ws + WS_PART); float* ml = (float*)(P.ws + WS_ML);
    part[(size_t)wg * 512 + tid] = acc;
    if (d == 0) { ml[(wg * 4 + h) * 2] = M; ml[(wg * 4 + h) * 2 + 1] = L; }
  }
}

__global__ void __launch_bounds__(NTHR) k_oproj1(Ptrs P) {
  extern __shared__ __attribute__((aligned(16))) unsigned char smem[];
  const int tid = threadIdx.x, lane = tid & 63, wave = __builtin_amdgcn_readfirstlane(tid >> 6);
  const int br = blockIdx.x >> 7, head = (blockIdx.x >> 2) & 31, cbk = blockIdx.x & 3;
  const int kvh = head >> 2, h = head & 3;
  const int g = lane >> 4, r16 = lane & 15, q4 = r16 >> 2, p4 = lane & 3;
  const unsigned lds0 = (unsigned)(size_t)(LAS unsigned char*)smem;
  {
    const unsigned char* sb = (const unsigned char*)P.wo[br] + (size_t)(head * 128 + 16 * wave) * 1024 + cbk * 256;
    unsigned vo[4];
#pragma unroll
    for (int p = 0; p < 4; ++p) vo[p] = (unsigned)((4 * p + g) * 1024 + ((r16 ^ (((4 * p + g) & 7) << 1)) * 16) - 1024 * p);
    dma4(lds0 + wave * 4096, sb, vo[0], vo[1], vo[2], vo[3]);
  }
  uint16_t* ob = (uint16_t*)(smem + 32768);
  {
    const float* part = (const float*)(P.ws + WS_PART); const float* ml = (const float*)(P.ws + WS_ML);
    const int bb = tid >> 6, d = (tid & 63) * 2;
    const int wg0 = (br << 7) + ((bb * 8 + kvh) << 1);
    const float m0 = ml[(wg0 * 4 + h) * 2], l0 = ml[(wg0 * 4 + h) * 2 + 1], m1 = ml[((wg0 + 1) * 4 + h) * 2], l1 = ml[((wg0 + 1) * 4 + h) * 2 + 1];
    const float M = fmaxf(m0, m1), w0 = __builtin_amdgcn_exp2f((m0 - M) * SCALE_LOG2E), w1 = __builtin_amdgcn_exp2f((m1 - M) * SCALE_LOG2E);
    const float inv = 1.f / (w0 * l0 + w1 * l1);
    const float2 a0 = *(const float2*)(part + (size_t)wg0 * 512 + h * 128 + d), a1 = *(const float2*)(part + (size_t)(wg0 + 1) * 512 + h * 128 + d);
    const float o0 = (w0 * a0.x + w1 * a1.x) * inv, o1 = (w0 * a0.y + w1 * a1.y) * inv;
    *(unsigned*)(ob + bb * 128 + d) = cvt_pk_bf16(o0, o1);
  }
  asm volatile("s_waitcnt vmcnt(0)" ::: "memory");
  __syncthreads();
  f32x4 acc = {0, 0, 0, 0};
#pragma unroll
  for (int s = 0; s < 4; ++s) {
    const int r0 = 32 * s + 8 * g + q4, r1 = r0 + 4;
    const unsigned a0 = lds0 + r0 * 256 + ((wave ^ (r0 & 7)) << 5) + p4 * 8, a1 = lds0 + r1 * 256 + ((wave ^ (r1 & 7)) << 5) + p4 * 8;
    bf16x4 lo, hi;
    asm volatile("ds_read_b64_tr_b16 %0, %2\n\tds_read_b64_tr_b16 %1, %3\n\ts_waitcnt lgkmcnt(0)" : "=&v"(lo), "=&v"(hi) : "v"(a0), "v"(a1) : "memory");
    const bf16x8 a = __builtin_shufflevector(lo, hi, 0, 1, 2, 3, 4, 5, 6, 7);
    const bf16x8 bfr = *(const bf16x8*)(ob + (r16 & 7) * 128 + 32 * s + 8 * g);
    acc = MFMA16(a, bfr, acc);
  }
  if (r16 < 8) {
    float* p2 = (float*)(P.ws + WS_P2);
    *(f32x4*)(p2 + ((size_t)(br * 32 + head) * 8 + r16) * 512 + cbk * 128 + wave * 16 + 4 * g) = acc;
  }
}

__global__ void __launch_bounds__(256) k_oproj2(Ptrs P) {
  const int idx = blockIdx.x * 256 + threadIdx.x;
  const int br = idx >> 12, b = (idx >> 9) & 7, col = idx & 511;
  const float* p2 = (const float*)(P.ws + WS_P2);
  float s = 0.f;
#pragma unroll
  for (int hd = 0; hd < 32; ++hd) s += p2[((size_t)(br * 32 + hd) * 8 + b) * 512 + col];
  P.out[b * 1024 + br * 512 + col] = (uint16_t)(cvt_pk_bf16(s, 0.f) & 0xffff);
}

extern "C" void kernel_launch(void* const* d_in, const int* in_sizes, int n_in, void* d_out, int out_size, void* d_ws, size_t ws_size, hipStream_t stream) {
  Ptrs P;
  for (int i = 0; i < 2; ++i) {
    P.x[i] = (const uint16_t*)d_in[5 * i + 0]; P.k[i] = (const uint16_t*)d_in[5 * i + 1]; P.v[i] = (const uint16_t*)d_in[5 * i + 2];
    P.wq[i] = (const uint16_t*)d_in[5 * i + 3]; P.wo[i] = (const uint16_t*)d_in[5 * i + 4];
  }
  P.out = (uint16_t*)d_out; P.ws = (unsigned char*)d_ws;
  (void)hipFuncSetAttribute((const void*)k_attn, hipFuncAttributeMaxDynamicSharedMemorySize, 131072);
  k_qproj<<<256, NTHR, 32768, stream>>>(P);
  k_attn<<<NWG, NTHR, 131072, stream>>>(P);
  k_oproj1<<<256, NTHR, 32768 + 2048, stream>>>(P);
  k_oproj2<<<32, 256, 0, stream>>>(P);
}
```

```cpp
#include <hip/hip_runtime.h>
#include <hip/hip_bf16.h>
#include <stdint.h>

#define LAS __attribute__((address_space(3)))
#define GAS __attribute__((address_space(1)))
typedef __attribute__((ext_vector_type(8))) short bf16x8;
typedef __attribute__((ext_vector_type(4))) short bf16x4;
typedef __attribute__((ext_vector_type(4))) float f32x4;
typedef __attribute__((ext_vector_type(2))) float f32x2;
typedef __attribute__((ext_vector_type(4))) unsigned u32x4;
typedef __attribute__((ext_vector_type(2))) __bf16 bf16x2_t;
typedef GAS unsigned gu32;
typedef GAS unsigned long long gu64;
#define RLX_AGENT __ATOMIC_RELAXED, __HIP_MEMORY_SCOPE_AGENT

constexpr int NB = 8, DM = 512, SK = 8192, DH = 128;
constexpr int NWG = 256, SW = 7, NTHR = 512;
constexpr int QTILES = 2 * SK / 32;
#define NSTAT 88
constexpr float SCALE_LOG2E = 0.08838834764831845f * 1.4426950408889634f;
constexpr unsigned long long TAG = 1ull << 32;
constexpr unsigned SPIN_CAP = 1u << 16;
#define POLL_SLEEP 4

constexpr size_t WS_READY = 0;
constexpr size_t WS_GO = 4096;
constexpr size_t WS_CNT = 40960;
constexpr unsigned WS_QIN = 65536;
constexpr unsigned WS_OA = WS_QIN + 256 * 512 * 4;
constexpr int OA_H = 132;
constexpr unsigned WS_OB = WS_OA + 256 * 8 * OA_H * 4;
constexpr unsigned WS_END = WS_OB + 256 * 1024 * 4;
constexpr size_t WS_DBG = 4u << 20;
constexpr unsigned EMPTY = 0xFFFFFFFFu;

constexpr int LDS_W = SW * 16384;
constexpr int LDS_PART = LDS_W + 32768;
constexpr int LDS_Q = LDS_PART + 8192;
constexpr int LDS_OB = LDS_Q + 2048;
constexpr int LDS_CTL = LDS_OB + 2048;
constexpr int LDS_TOTAL = LDS_CTL + 64;

__device__ __forceinline__ unsigned cvt_pk_bf16(float lo, float hi) {
  f32x2 v = {lo, hi};
  return __builtin_bit_cast(unsigned, __builtin_convertvector(v, bf16x2_t));
}
#define MFMA16(a, b, c) __builtin_amdgcn_mfma_f32_16x16x32_bf16((a), (b), (c), 0, 0, 0)

__device__ __forceinline__ void dma4(unsigned lds_dst, const void* sbase, unsigned v0, unsigned v1, unsigned v2, unsigned v3) {
  unsigned keep;
  asm volatile("s_mov_b32 %0, m0\n\ts_mov_b32 m0, %1\n\ts_nop 3\n\t"
               "global_load_lds_dwordx4 %3, %2 offset:0 nt\n\t"
               "global_load_lds_dwordx4 %4, %2 offset:1024 nt\n\t"
               "global_load_lds_dwordx4 %5, %2 offset:2048 nt\n\t"
               "global_load_lds_dwordx4 %6, %2 offset:3072 nt\n\t"
               "s_mov_b32 m0, %0"
               : "=&s"(keep) : "s"(lds_dst), "s"(sbase), "v"(v0), "v"(v1), "v"(v2), "v"(v3) : "memory");
}
__device__ __forceinline__ void dma1(unsigned lds_dst, const void* sbase, unsigned v0) {
  unsigned keep;
  asm volatile("s_mov_b32 %0, m0\n\ts_mov_b32 m0, %1\n\ts_nop 3\n\t"
               "global_load_lds_dwordx4 %3, %2 offset:0\n\t"
               "s_mov_b32 m0, %0"
               : "=&s"(keep) : "s"(lds_dst), "s"(sbase), "v"(v0) : "memory");
}
#define BAR() asm volatile("s_waitcnt lgkmcnt(0)\n\ts_barrier" ::: "memory")
#define VMCNT_(n) asm volatile("s_waitcnt vmcnt(" #n ")" ::: "memory")
#define VMCNT(n) VMCNT_(n)

__device__ __forceinline__ float xg_max(float x) {
  auto s = __builtin_amdgcn_permlane16_swap(__float_as_uint(x), __float_as_uint(x), false, false);
  x = fmaxf(__uint_as_float(s[0]), __uint_as_float(s[1]));
  auto t = __builtin_amdgcn_permlane32_swap(__float_as_uint(x), __float_as_uint(x), false, false);
  return fmaxf(__uint_as_float(t[0]), __uint_as_float(t[1]));
}
__device__ __forceinline__ float xg_sum(float x) {
  auto s = __builtin_amdgcn_permlane16_swap(__float_as_uint(x), __float_as_uint(x), false, false);
  x = __uint_as_float(s[0]) + __uint_as_float(s[1]);
  auto t = __builtin_amdgcn_permlane32_swap(__float_as_uint(x), __float_as_uint(x), false, false);
  return __uint_as_float(t[0]) + __uint_as_float(t[1]);
}
__device__ __forceinline__ void st64(gu64* p, unsigned long long v) { __hip_atomic_store(p, v, RLX_AGENT); }
__device__ __forceinline__ unsigned long long ld64(gu64* p) { return __hip_atomic_load(p, RLX_AGENT); }
__device__ __forceinline__ void st32(gu32* p, unsigned v) { __hip_atomic_store(p, v, RLX_AGENT); }
__device__ __forceinline__ unsigned ld32(gu32* p) { return __hip_atomic_load(p, RLX_AGENT); }
__device__ __forceinline__ unsigned pay(unsigned u) { return u == EMPTY ? 0x7FC07FC0u : u; }
__device__ __forceinline__ unsigned pay(float v) { return pay(__float_as_uint(v)); }
__device__ __forceinline__ void st128(__amdgpu_buffer_rsrc_t rs, unsigned off, u32x4 v) { __builtin_amdgcn_raw_buffer_store_b128(v, rs, off, 0, 16); }
__device__ __forceinline__ u32x4 ld128(__amdgpu_buffer_rsrc_t rs, unsigned off) { asm volatile("" ::: "memory"); return __builtin_amdgcn_raw_buffer_load_b128(rs, off, 0, 16); }
__device__ __forceinline__ bool full(u32x4 v) { return v[0] != EMPTY && v[1] != EMPTY && v[2] != EMPTY && v[3] != EMPTY; }

struct Ptrs {
  const uint16_t* x[2]; const uint16_t* k[2]; const uint16_t* v[2]; const uint16_t* wq[2]; const uint16_t* wo[2];
  uint16_t* out; unsigned char* ws;
};

#define STAMP(i) do {} while (0)

struct KFrag { bf16x8 f[8]; };
struct VFrag { bf16x4 lo[8], hi[8]; };

__device__ __forceinline__ void readK(KFrag& k, const unsigned (&ak)[4]) {
  asm volatile("ds_read_b128 %0, %8\n\tds_read_b128 %1, %9\n\tds_read_b128 %2, %10\n\tds_read_b128 %3, %11\n\t"
               "ds_read_b128 %4, %8 offset:4096\n\tds_read_b128 %5, %9 offset:4096\n\tds_read_b128 %6, %10 offset:4096\n\tds_read_b128 %7, %11 offset:4096\n\t"
               "s_waitcnt lgkmcnt(0)"
               : "=&v"(k.f[0]), "=&v"(k.f[1]), "=&v"(k.f[2]), "=&v"(k.f[3]), "=&v"(k.f[4]), "=&v"(k.f[5]), "=&v"(k.f[6]), "=&v"(k.f[7])
               : "v"(ak[0]), "v"(ak[1]), "v"(ak[2]), "v"(ak[3]) : "memory");
}
__device__ __forceinline__ void readV(VFrag& v, unsigned av0) {
#pragma unroll
  for (int hb = 0; hb < 2; ++hb) {
    const unsigned a0 = av0 ^ ((4 * hb + 0) << 5), a1 = av0 ^ ((4 * hb + 1) << 5), a2 = av0 ^ ((4 * hb + 2) << 5), a3 = av0 ^ ((4 * hb + 3) << 5);
    asm volatile("ds_read_b64_tr_b16 %0, %8\n\tds_read_b64_tr_b16 %1, %9\n\tds_read_b64_tr_b16 %2, %10\n\tds_read_b64_tr_b16 %3, %11\n\t"
                 "ds_read_b64_tr_b16 %4, %8 offset:4096\n\tds_read_b64_tr_b16 %5, %9 offset:4096\n\tds_read_b64_tr_b16 %6, %10 offset:4096\n\tds_read_b64_tr_b16 %7, %11 offset:4096\n\t"
                 "s_waitcnt lgkmcnt(0)"
                 : "=&v"(v.lo[4 * hb + 0]), "=&v"(v.lo[4 * hb + 1]), "=&v"(v.lo[4 * hb + 2]), "=&v"(v.lo[4 * hb + 3]),
                   "=&v"(v.hi[4 * hb + 0]), "=&v"(v.hi[4 * hb + 1]), "=&v"(v.hi[4 * hb + 2]), "=&v"(v.hi[4 * hb + 3])
                 : "v"(a0), "v"(a1), "v"(a2), "v"(a3) : "memory");
  }
}
__device__ __forceinline__ void tile_compute(const KFrag& k, const VFrag& v, const bf16x8 (&qf)[4], float& m, float& l, f32x4 (&o)[8]) {
  f32x4 s0 = {0, 0, 0, 0}, s1 = {0, 0, 0, 0};
#pragma unroll
  for (int s = 0; s < 4; ++s) { s0 = MFMA16(k.f[s], qf[s], s0); s1 = MFMA16(k.f[4 + s], qf[s], s1); }
  float mx = fmaxf(fmaxf(fmaxf(s0[0], s0[1]), fmaxf(s0[2], s0[3])), fmaxf(fmaxf(s1[0], s1[1]), fmaxf(s1[2], s1[3])));
  mx = xg_max(mx);
  const float mn = fmaxf(m, mx);
  const float alpha = __builtin_amdgcn_exp2f((m - mn) * SCALE_LOG2E);
  const float mc = mn * SCALE_LOG2E;
  float p[8];
#pragma unroll
  for (int jj = 0; jj < 4; ++jj) { p[jj] = __builtin_amdgcn_exp2f(fmaf(s0[jj], SCALE_LOG2E, -mc)); p[4 + jj] = __builtin_amdgcn_exp2f(fmaf(s1[jj], SCALE_LOG2E, -mc)); }
  l = l * alpha + (((p[0] + p[1]) + (p[2] + p[3])) + ((p[4] + p[5]) + (p[6] + p[7])));
  m = mn;
  u32x4 pk = {cvt_pk_bf16(p[0], p[1]), cvt_pk_bf16(p[2], p[3]), cvt_pk_bf16(p[4], p[5]), cvt_pk_bf16(p[6], p[7])};
  const bf16x8 pb = __builtin_bit_cast(bf16x8, pk);
#pragma unroll
  for (int blk = 0; blk < 8; ++blk) {
    o[blk] *= alpha;
    const bf16x8 a = __builtin_shufflevector(v.lo[blk], v.hi[blk], 0, 1, 2, 3, 4, 5, 6, 7);
    o[blk] = MFMA16(a, pb, o[blk]);
  }
}

__global__ void __launch_bounds__(NTHR, 2) k_fused(Ptrs P) {
  extern __shared__ __attribute__((aligned(16))) unsigned char smem[];
  const int tid = threadIdx.x, lane = tid & 63, wave = __builtin_amdgcn_readfirstlane(tid >> 6);
  const int bid = blockIdx.x, pos = bid & 7;
  const int quad = (bid >> 3) * 2 + ((pos >> 1) & 1), r = (pos & 1) | ((pos >> 2) << 1);
  const int wg = quad * 4 + r;
  const int br = quad >> 5, kvh = (quad >> 2) & 7, qi = quad & 3;
  const int j = qi * 4 + r;
  const int grp = wg & ~15;
  const int g = lane >> 4, r16 = lane & 15, q4 = r16 >> 2, p4 = lane & 3;
  const unsigned lds0 = (unsigned)(size_t)(LAS unsigned char*)smem;
  const __amdgpu_buffer_rsrc_t rs = __builtin_amdgcn_make_buffer_rsrc(P.ws, 0, WS_END, 0x00020000);
  const u32x4 EMPTY4 = {EMPTY, EMPTY, EMPTY, EMPTY};
  gu32* const READY = (gu32*)(P.ws + WS_READY);
  gu32* const GO = (gu32*)(P.ws + WS_GO);
  gu32* const CNT = (gu32*)(P.ws + WS_CNT) + quad * 32;
  const unsigned qin = WS_QIN + wg * 2048 + lane * 16;
  volatile LAS unsigned* const ctl = (volatile LAS unsigned*)(smem + LDS_CTL);
  const int th = j >> 2, cbk = j & 3, hd = kvh * 4 + th;
  const bool svc = wave == 0;
  const bool leader = wg == 0;
  const int sw = wave - 1;
  const unsigned slotK = lds0 + sw * 16384, slotV = slotK + 8192;
  const size_t seq0 = ((size_t)(2 * qi) * 8 + kvh) * SK * 256;
  const unsigned char* kbase = (const unsigned char*)P.k[br] + seq0;
  const unsigned char* vbase = (const unsigned char*)P.v[br] + seq0;
  unsigned vk[4], vv[2];
#pragma unroll
  for (int p = 0; p < 4; ++p) vk[p] = (unsigned)(g * 256 + ((r16 ^ ((4 * p + g) & 15)) * 16));
#pragma unroll
  for (int p = 0; p < 2; ++p) vv[p] = (unsigned)(g * 256 + ((r16 ^ (((4 * p + g) & 7) << 1)) * 16));
  auto toff = [&](int t) { return (size_t)(t >> 8) * ((size_t)8 * SK * 256) + (size_t)(t & 255) * 8192; };
  auto issueK = [&](int t, int hi) { dma4(slotK + hi * 4096, kbase + toff(t) + hi * 4096, vk[0], vk[1], vk[2], vk[3]); };
  auto issueV = [&](int t, int hi) { dma4(slotV + hi * 4096, vbase + toff(t) + hi * 4096, vv[0], vv[1], vv[0], vv[1]); };

  STAMP(0);
  if (svc) {
    __builtin_amdgcn_s_setprio(3);
    st128(rs, qin, EMPTY4); st128(rs, qin + 1024, EMPTY4);
    if (lane == 0) st32(GO + wg * 32, 0u);
    if (leader) st128(rs, WS_READY + lane * 16, u32x4{0u, 0u, 0u, 0u});
    if (lane == 0) { ctl[0] = 3 * SW; ctl[1] = 0u; ctl[2] = 0u; }
  }
  {
    const unsigned char* sb = (const unsigned char*)P.wq[br] + (size_t)(64 * wave) * 8192 + (kvh * 512 + 32 * j) * 2;
    unsigned vo[4];
#pragma unroll
    for (int p = 0; p < 4; ++p) vo[p] = (unsigned)((16 * p + (lane >> 2)) * 8192 + (lane & 3) * 16 - 1024 * p);
    dma4(lds0 + LDS_W + wave * 4096, sb, vo[0], vo[1], vo[2], vo[3]);
  }
  dma1(lds0 + LDS_PART + wave * 1024, (const unsigned char*)P.x[br] + 128 * wave, (unsigned)((lane >> 3) * (DM * 2) + (lane & 7) * 16));
  const int t0 = 4 * sw + r, t1 = 4 * (SW + sw) + r, t2 = 4 * (2 * SW + sw) + r;
  if (!svc) {
    issueK(t0, 0);
    VMCNT(4);
    issueK(t0, 1);
  } else {
    VMCNT(0);
  }
  STAMP(1);
  {
    f32x4 acc0 = {0, 0, 0, 0}, acc1 = {0, 0, 0, 0};
    const unsigned a0 = lds0 + LDS_W + wave * 4096 + (8 * g + q4) * 64 + p4 * 16;
    const unsigned ax = lds0 + LDS_PART + wave * 1024 + (r16 & 7) * 128 + g * 16;
    bf16x4 w[8]; bf16x8 xb0, xb1;
    asm volatile("ds_read_b64_tr_b16 %0, %10\n\tds_read_b64_tr_b16 %1, %10 offset:256\n\t"
                 "ds_read_b64_tr_b16 %2, %10 offset:8\n\tds_read_b64_tr_b16 %3, %10 offset:264\n\t"
                 "ds_read_b64_tr_b16 %4, %10 offset:2048\n\tds_read_b64_tr_b16 %5, %10 offset:2304\n\t"
                 "ds_read_b64_tr_b16 %6, %10 offset:2056\n\tds_read_b64_tr_b16 %7, %10 offset:2312\n\t"
                 "ds_read_b128 %8, %11\n\tds_read_b128 %9, %11 offset:64\n\t"
                 "s_waitcnt lgkmcnt(0)"
                 : "=&v"(w[0]), "=&v"(w[1]), "=&v"(w[2]), "=&v"(w[3]), "=&v"(w[4]), "=&v"(w[5]), "=&v"(w[6]), "=&v"(w[7]), "=&v"(xb0), "=&v"(xb1) : "v"(a0), "v"(ax) : "memory");
    acc0 = MFMA16(__builtin_shufflevector(w[0], w[1], 0, 1, 2, 3, 4, 5, 6, 7), xb0, acc0);
    acc1 = MFMA16(__builtin_shufflevector(w[2], w[3], 0, 1, 2, 3, 4, 5, 6, 7), xb0, acc1);
    acc0 = MFMA16(__builtin_shufflevector(w[4], w[5], 0, 1, 2, 3, 4, 5, 6, 7), xb1, acc0);
    acc1 = MFMA16(__builtin_shufflevector(w[6], w[7], 0, 1, 2, 3, 4, 5, 6, 7), xb1, acc1);
    if (r16 < 8) {
      f32x4* part = (f32x4*)(smem + LDS_PART + wave * 1024);
      part[g * 8 + r16] = acc0; part[32 + g * 8 + r16] = acc1;
    }
  }
  BAR();

  if (!svc) {
    unsigned ak[4], av0;
#pragma unroll
    for (int s = 0; s < 4; ++s) ak[s] = slotK + r16 * 256 + (((4 * s + g) ^ r16) * 16);
    av0 = slotV + (4 * g + q4) * 256 + (((4 * g + q4) & 7) << 5) + p4 * 8;
#define STEP(A, B) do { if (__builtin_amdgcn_readfirstlane(ctl[1])) { A; B; VMCNT(8); } else { VMCNT(4); A; VMCNT(4); B; } } while (0)
#define TREC() do {} while (0)
    KFrag kA, kB; VFrag vA, vB;
    STEP(issueV(t0, 0), issueV(t0, 1));
    readK(kA, ak);
    STEP(issueK(t1, 0), issueK(t1, 1));
    readV(vA, av0); TREC();
    STEP(issueV(t1, 0), issueV(t1, 1));
    readK(kB, ak);
    STEP(issueK(t2, 0), issueK(t2, 1));
    readV(vB, av0); TREC();
    { VMCNT(4); issueV(t2, 0); VMCNT(4); issueV(t2, 1); }
    STAMP(7);
    while (!__builtin_amdgcn_readfirstlane(ctl[1])) __builtin_amdgcn_s_sleep(1);
    asm volatile("" ::: "memory");
    STAMP(2);
    bf16x8 qf[4];
    auto load_q = [&](int sq) {
      const uint16_t* qp = (const uint16_t*)(smem + LDS_Q) + sq * 512 + (r16 & 3) * DH + 8 * g;
#pragma unroll
      for (int s = 0; s < 4; ++s) { bf16x8 t = *(const bf16x8*)(qp + 32 * s); if (r16 >= 4) t = bf16x8{0, 0, 0, 0, 0, 0, 0, 0}; qf[s] = t; }
    };
    load_q(0);
    float m = -INFINITY, l = 0.f, mA = -INFINITY, lA = 0.f;
    f32x4 o[8], oA[8];
#pragma unroll
    for (int i = 0; i < 8; ++i) o[i] = f32x4{0, 0, 0, 0};
    int pend = 0; bool pend_dyn = false;
    unsigned areg = 0;
    auto claim = [&]() {
      unsigned tk = 0;
      if (lane == 0) tk = __hip_atomic_fetch_add((LAS unsigned*)(smem + LDS_CTL), 1u, __ATOMIC_RELAXED, __HIP_MEMORY_SCOPE_WORKGROUP);
      const int n = __builtin_amdgcn_readfirstlane(tk);
      pend = 4 * n + r; pend_dyn = n >= NSTAT;
      if (pend_dyn) { while (!__builtin_amdgcn_readfirstlane(ctl[2])) __builtin_amdgcn_s_sleep(1); }
      unsigned long long sv; const unsigned zero = 0, one = 1; const unsigned long long em = pend_dyn ? 1ull : 0ull;
      asm volatile("s_mov_b64 %0, exec\n\ts_and_b64 exec, exec, %5\n\ts_cbranch_execz 1f\n\tglobal_atomic_add %1, %2, %3, %4 sc0\n1:\n\ts_mov_b64 exec, %0"
                   : "=&s"(sv), "+v"(areg) : "v"(zero), "v"(one), "s"(CNT), "s"(em) : "memory", "scc");
    };
    claim();
    int nxt; bool more;
    auto fetch = [&](KFrag& kc, VFrag& vc) {
      asm volatile("s_waitcnt vmcnt(8)" : "+v"(areg) :: "memory");
      nxt = pend_dyn ? 4 * NSTAT + (int)__builtin_amdgcn_readfirstlane(areg) : pend;
      readK(kc, ak);
      more = (unsigned)nxt < (unsigned)QTILES;
      claim();
      if (more) {
        issueK(nxt, 0); issueK(nxt, 1);
        if (pend_dyn) VMCNT(9); else VMCNT(8);
      } else VMCNT(0);
      readV(vc, av0); TREC();
      if (more) { issueV(nxt, 0); issueV(nxt, 1); }
    };
    {
      KFrag kc; VFrag vc;
      fetch(kc, vc);
      tile_compute(kA, vA, qf, m, l, o); tile_compute(kB, vB, qf, m, l, o); tile_compute(kc, vc, qf, m, l, o);
    }
    int curseq = 0;
#pragma unroll
    for (int i = 0; i < 8; ++i) oA[i] = f32x4{0, 0, 0, 0};
    while (more) {
      const int cur = nxt;
      KFrag kc; VFrag vc;
      fetch(kc, vc);
      if ((cur >> 8) != curseq) {
        mA = m; lA = l; m = -INFINITY; l = 0.f;
#pragma unroll
        for (int i = 0; i < 8; ++i) { oA[i] = o[i]; o[i] = f32x4{0, 0, 0, 0}; }
        load_q(1);
        curseq = 1;
      }
      tile_compute(kc, vc, qf, m, l, o);
    }
    if (curseq == 0) {
      mA = m; lA = l; m = -INFINITY; l = 0.f;
#pragma unroll
      for (int i = 0; i < 8; ++i) { oA[i] = o[i]; o[i] = f32x4{0, 0, 0, 0}; }
    }
    STAMP(3);
    l = xg_sum(l); lA = xg_sum(lA);
    float* mo = (float*)(smem + sw * 16384);
    if (r16 < 4) {
#pragma unroll
      for (int blk = 0; blk < 8; ++blk) { *(f32x4*)(mo + r16 * 128 + 16 * blk + 4 * g) = oA[blk]; *(f32x4*)(mo + 512 + r16 * 128 + 16 * blk + 4 * g) = o[blk]; }
      if (g == 0) { mo[1024 + r16 * 2] = mA; mo[1024 + r16 * 2 + 1] = lA; mo[1032 + r16 * 2] = m; mo[1032 + r16 * 2 + 1] = l; }
    }
  } else {
    u32x4 pk;
    {
      const int ls = g * 8 + (lane & 7);
      f32x4 s0 = {0, 0, 0, 0}, s1 = {0, 0, 0, 0};
#pragma unroll
      for (int w = 0; w < 8; ++w) { const f32x4* part = (const f32x4*)(smem + LDS_PART + w * 1024); s0 += part[ls]; s1 += part[32 + ls]; }
      pk[0] = pay(cvt_pk_bf16(s0[0], s0[1])); pk[1] = pay(cvt_pk_bf16(s0[2], s0[3])); pk[2] = pay(cvt_pk_bf16(s1[0], s1[1])); pk[3] = pay(cvt_pk_bf16(s1[2], s1[3]));
    }
    const int bq = lane & 7;
    const unsigned qdst = WS_QIN + (grp + (bq >> 1) * 4 + ((lane >> 3) & 1) * 2) * 2048 + (bq & 1) * 1024 + j * 64 + g * 16;
    auto send_q = [&]() { st128(rs, qdst, pk); st128(rs, qdst + 2048, pk); };
    send_q();
    STAMP(2);
    bool q_done = false, go_seen = false, lead_done = !leader, cleared = false;
    for (unsigned spins = 0;; ++spins) {
      if (!q_done) {
        const u32x4 x0 = ld128(rs, qin), x1 = ld128(rs, qin + 1024);
        if (__all(full(x0) && full(x1)) || spins > SPIN_CAP) {
          ((u32x4*)(smem + LDS_Q))[lane] = x0; ((u32x4*)(smem + LDS_Q))[64 + lane] = x1;
          asm volatile("s_waitcnt lgkmcnt(0)" ::: "memory");
          if (lane == 0) ctl[1] = 1u;
          STAMP(3);
          q_done = true;
        }
      }
      if (q_done || spins >= 8) {
        if (!cleared) {
          for (int i = lane; i < 8 * OA_H / 4; i += 64) st128(rs, WS_OA + wg * (8 * OA_H * 4) + i * 16, EMPTY4);
#pragma unroll
          for (int k = 0; k < 4; ++k) st128(rs, WS_OB + wg * 4096 + (lane + 64 * k) * 16, EMPTY4);
          if (r == 0 && lane == 0) st32(CNT, 0u);
          send_q();
          VMCNT(0);
          cleared = true;
        }
        if (!go_seen) {
          if (lane == 0) st32(READY + wg, 1u);
          if (!lead_done) {
            const u32x4 rr = ld128(rs, WS_READY + lane * 16);
            if (__all((rr[0] & rr[1] & rr[2] & rr[3]) == 1u) || spins > SPIN_CAP) {
#pragma unroll
              for (int k = 0; k < 4; ++k) st32(GO + (lane + 64 * k) * 32, 1u);
              lead_done = true;
            }
          }
          const unsigned gv = __builtin_amdgcn_readfirstlane(ld32(GO + wg * 32));
          if (gv == 1u || spins > SPIN_CAP) { go_seen = true; if (lane == 0) ctl[2] = 1u; send_q(); STAMP(8); }
        }
        if (q_done && go_seen) break;
        __builtin_amdgcn_s_sleep(32);
      } else if (spins & 1) {
        send_q();
      }
    }
    {
      const unsigned char* sb = (const unsigned char*)P.wo[br] + (size_t)(hd * 128) * 1024 + cbk * 256;
      unsigned vo[4];
#pragma unroll
      for (int p = 0; p < 4; ++p) vo[p] = (unsigned)((4 * p + g) * 1024 + ((r16 ^ (((4 * p + g) & 7) << 1)) * 16) - 1024 * p);
#pragma unroll
      for (int c = 0; c < 8; ++c) dma4(lds0 + LDS_W + c * 4096, sb + c * 16384, vo[0], vo[1], vo[2], vo[3]);
    }
    VMCNT(0);
  }
  BAR();
  STAMP(4);
  if (tid < 256) {
    const int sq = tid >> 7, h = (tid >> 5) & 3, d4 = (tid & 31) * 4;
    float mw[SW], M = -INFINITY;
#pragma unroll
    for (int w = 0; w < SW; ++w) { mw[w] = ((const float*)(smem + w * 16384))[1024 + sq * 8 + h * 2]; M = fmaxf(M, mw[w]); }
    const float Ms = (M == -INFINITY) ? 0.f : M;
    float L = 0.f; f32x4 acc = {0, 0, 0, 0};
#pragma unroll
    for (int w = 0; w < SW; ++w) {
      const float* mo = (const float*)(smem + w * 16384);
      const float wt = __builtin_amdgcn_exp2f((mw[w] - Ms) * SCALE_LOG2E);
      L = fmaf(wt, mo[1024 + sq * 8 + h * 2 + 1], L);
      acc += wt * *(const f32x4*)(mo + sq * 512 + h * 128 + d4);
    }
    const unsigned oa = WS_OA + (wg * 8 + sq * 4 + h) * (OA_H * 4);
    st128(rs, oa + d4 * 4, u32x4{pay(acc[0]), pay(acc[1]), pay(acc[2]), pay(acc[3])});
    if (d4 == 0) st128(rs, oa + 512, u32x4{pay(M), pay(L), 0u, 0u});
  }
  bf16x4 wlo[4], whi[4];
  {
    unsigned a0[4], a1[4];
#pragma unroll
    for (int s = 0; s < 4; ++s) {
      const int r0 = 32 * s + 8 * g + q4, r1 = r0 + 4;
      a0[s] = lds0 + LDS_W + r0 * 256 + ((wave ^ (r0 & 7)) << 5) + p4 * 8;
      a1[s] = lds0 + LDS_W + r1 * 256 + ((wave ^ (r1 & 7)) << 5) + p4 * 8;
    }
    asm volatile("ds_read_b64_tr_b16 %0, %8\n\tds_read_b64_tr_b16 %1, %9\n\tds_read_b64_tr_b16 %2, %10\n\tds_read_b64_tr_b16 %3, %11\n\t"
                 "ds_read_b64_tr_b16 %4, %12\n\tds_read_b64_tr_b16 %5, %13\n\tds_read_b64_tr_b16 %6, %14\n\tds_read_b64_tr_b16 %7, %15\n\t"
                 "s_waitcnt lgkmcnt(0)"
                 : "=&v"(wlo[0]), "=&v"(wlo[1]), "=&v"(wlo[2]), "=&v"(wlo[3]), "=&v"(whi[0]), "=&v"(whi[1]), "=&v"(whi[2]), "=&v"(whi[3])
                 : "v"(a0[0]), "v"(a0[1]), "v"(a0[2]), "v"(a0[3]), "v"(a1[0]), "v"(a1[1]), "v"(a1[2]), "v"(a1[3]) : "memory");
  }
  {
    const unsigned a0 = WS_OA + ((grp + (wave >> 1) * 4) * 8 + (wave & 1) * 4 + th) * (OA_H * 4);
    constexpr unsigned MS = 8 * OA_H * 4;
    u32x4 ml = {0u, 0u, 0u, 0u};
    {
      bool seen = false;
      for (unsigned spins = 0;; ++spins) {
        if (!seen && lane < 4) { ml = ld128(rs, a0 + lane * MS + 512); seen = full(ml); }
        if (__all(seen || lane >= 4) || spins > SPIN_CAP) break;
        __builtin_amdgcn_s_sleep(POLL_SLEEP);
      }
    }
    u32x4 xa, xb;
    for (unsigned spins = 0;; ++spins) {
      xa = ld128(rs, a0 + (lane >> 5) * MS + (lane & 31) * 16); xb = ld128(rs, a0 + (2 + (lane >> 5)) * MS + (lane & 31) * 16);
      if (__all(full(xa) && full(xb)) || spins > SPIN_CAP) break;
      __builtin_amdgcn_s_sleep(1);
    }
    float mc[4], lc[4], M = -INFINITY;
#pragma unroll
    for (int c = 0; c < 4; ++c) { mc[c] = __uint_as_float(__builtin_amdgcn_readlane(ml[0], c)); lc[c] = __uint_as_float(__builtin_amdgcn_readlane(ml[1], c)); M = fmaxf(M, mc[c]); }
    float wt[4], L = 0.f;
#pragma unroll
    for (int c = 0; c < 4; ++c) { wt[c] = __builtin_amdgcn_exp2f((mc[c] - M) * SCALE_LOG2E); L = fmaf(wt[c], lc[c], L); }
    const float inv = 1.f / L;
    const float wa = (lane < 32 ? wt[0] : wt[1]) * inv, wb = (lane < 32 ? wt[2] : wt[3]) * inv;
    float ov[4];
#pragma unroll
    for (int e = 0; e < 4; ++e) {
      const float t = fmaf(wa, __uint_as_float(xa[e]), wb * __uint_as_float(xb[e]));
      auto sw2 = __builtin_amdgcn_permlane32_swap(__float_as_uint(t), __float_as_uint(t), false, false);
      ov[e] = __uint_as_float(sw2[0]) + __uint_as_float(sw2[1]);
    }
    if (lane < 32) *(uint2*)(smem + LDS_OB + wave * 256 + lane * 8) = make_uint2(cvt_pk_bf16(ov[0], ov[1]), cvt_pk_bf16(ov[2], ov[3]));
  }
  STAMP(5);
  BAR();
  {
    const uint16_t* ob = (const uint16_t*)(smem + LDS_OB);
    f32x4 acc = {0, 0, 0, 0};
#pragma unroll
    for (int s = 0; s < 4; ++s) {
      const bf16x8 a = __builtin_shufflevector(wlo[s], whi[s], 0, 1, 2, 3, 4, 5, 6, 7);
      const bf16x8 bfr = *(const bf16x8*)(ob + (r16 & 7) * 128 + 32 * s + 8 * g);
      acc = MFMA16(a, bfr, acc);
    }
    if (r16 < 8) st128(rs, WS_OB + wg * 4096 + (r16 * 128 + wave * 16 + 4 * g) * 4, u32x4{pay(acc[0]), pay(acc[1]), pay(acc[2]), pay(acc[3])});
  }
  if (svc) {
    const int fb = hd >> 2, fc = (hd & 3) * 32;
    const unsigned src = WS_OB + ((br << 7) + (lane >> 3) * 4 + cbk) * 4096 + (fb * 128 + fc + (lane & 7) * 4) * 4;
    u32x4 x[4];
    for (unsigned spins = 0;; ++spins) {
#pragma unroll
      for (int k = 0; k < 4; ++k) x[k] = ld128(rs, src + k * (32 * 4096));
      if (__all(full(x[0]) && full(x[1]) && full(x[2]) && full(x[3])) || spins > SPIN_CAP) break;
      __builtin_amdgcn_s_sleep(POLL_SLEEP);
    }
    float sv[4];
#pragma unroll
    for (int e = 0; e < 4; ++e) {
      float t = (__uint_as_float(x[0][e]) + __uint_as_float(x[1][e])) + (__uint_as_float(x[2][e]) + __uint_as_float(x[3][e]));
      t += __builtin_bit_cast(float, __builtin_amdgcn_mov_dpp(__builtin_bit_cast(int, t), 0x128, 0xf, 0xf, true));
      sv[e] = xg_sum(t);
    }
    if (lane < 8) *(uint2*)(P.out + fb * 1024 + br * 512 + cbk * 128 + fc + lane * 4) = make_uint2(cvt_pk_bf16(sv[0], sv[1]), cvt_pk_bf16(sv[2], sv[3]));
  }
  STAMP(6);
}

extern "C" void kernel_launch(void* const* d_in, const int* in_sizes, int n_in, void* d_out, int out_size, void* d_ws, size_t ws_size, hipStream_t stream) {
  Ptrs P;
  for (int i = 0; i < 2; ++i) {
    P.x[i] = (const uint16_t*)d_in[5 * i + 0]; P.k[i] = (const uint16_t*)d_in[5 * i + 1]; P.v[i] = (const uint16_t*)d_in[5 * i + 2];
    P.wq[i] = (const uint16_t*)d_in[5 * i + 3]; P.wo[i] = (const uint16_t*)d_in[5 * i + 4];
  }
  P.out = (uint16_t*)d_out; P.ws = (unsigned char*)d_ws;
  (void)hipFuncSetAttribute((const void*)k_fused, hipFuncAttributeMaxDynamicSharedMemorySize, LDS_TOTAL);
  k_fused<<<NWG, NTHR, LDS_TOTAL, stream>>>(P);
}
```

```cpp
#include <hip/hip_runtime.h>
#include <hip/hip_bf16.h>
#include <stdint.h>

#define LAS __attribute__((address_space(3)))
typedef __attribute__((ext_vector_type(8))) short bf16x8;
typedef __attribute__((ext_vector_type(4))) short bf16x4;
typedef __attribute__((ext_vector_type(4))) float f32x4;
typedef __attribute__((ext_vector_type(2))) float f32x2;
typedef __attribute__((ext_vector_type(4))) unsigned u32x4;
typedef __attribute__((ext_vector_type(2))) __bf16 bf16x2_t;

constexpr int DM = 512, SK = 8192, DH = 128;
constexpr int NWG = 256, SW = 7, NTHR = 512;
constexpr int TILES = SK / 2 / 32;
constexpr float SCALE_LOG2E = 0.08838834764831845f * 1.4426950408889634f;
constexpr unsigned SPIN_CAP = 1u << 16;
constexpr int POLL_SLEEP = 2;
constexpr int BIAS = 9;
constexpr int LEAD = 1;
constexpr int MISS = 2;
constexpr bool PACE = true;
constexpr unsigned PT0 = 1400, PT1 = 3000;
constexpr int PINIT = 7000;
constexpr float PSLOW = 8200.f;
constexpr float PEPS = -0.005f;
constexpr float PAHEAD = 2.0f;

constexpr unsigned IN_WG = 1408;
constexpr int OA_H = 132;
constexpr unsigned OA_USED = 4 * OA_H * 4, OA_WG = 2176, OA_SZ = 256 * OA_WG, OB_SZ = 256 * 4096;
constexpr unsigned WS_QIN = 0;
constexpr unsigned WS_OA = WS_QIN + 256 * IN_WG;
constexpr unsigned WS_OB = WS_OA + OA_SZ;
constexpr unsigned WS_END = WS_OB + OB_SZ;
constexpr unsigned EMPTY = 0xFFFFFFFFu;

constexpr int LDS_W = SW * 16384;
constexpr int LDS_X = LDS_W + 32768;
constexpr int LDS_Q = LDS_X + 8192;
constexpr int LDS_CTL = LDS_Q + 1024;
constexpr int LDS_TOTAL = LDS_CTL + 128;

__device__ __forceinline__ unsigned cvt_pk_bf16(float lo, float hi) {
  f32x2 v = {lo, hi};
  return __builtin_bit_cast(unsigned, __builtin_convertvector(v, bf16x2_t));
}
#define MFMA16(a, b, c) __builtin_amdgcn_mfma_f32_16x16x32_bf16((a), (b), (c), 0, 0, 0)

__device__ __forceinline__ void dma4(unsigned lds_dst, const void* sbase, unsigned v0, unsigned v1, unsigned v2, unsigned v3) {
  unsigned keep;
  asm volatile("s_mov_b32 %0, m0\n\ts_mov_b32 m0, %1\n\ts_nop 3\n\t"
               "global_load_lds_dwordx4 %3, %2 offset:0 nt\n\t"
               "global_load_lds_dwordx4 %4, %2 offset:1024 nt\n\t"
               "global_load_lds_dwordx4 %5, %2 offset:2048 nt\n\t"
               "global_load_lds_dwordx4 %6, %2 offset:3072 nt\n\t"
               "s_mov_b32 m0, %0"
               : "=&s"(keep) : "s"(lds_dst), "s"(sbase), "v"(v0), "v"(v1), "v"(v2), "v"(v3) : "memory");
}
__device__ __forceinline__ void dma4d(unsigned lds_dst, const void* sbase, unsigned v0, unsigned v1, unsigned v2, unsigned v3) {
  unsigned keep;
  asm volatile("s_mov_b32 %0, m0\n\ts_mov_b32 m0, %1\n\ts_nop 3\n\t"
               "global_load_lds_dwordx4 %3, %2 offset:0\n\t"
               "global_load_lds_dwordx4 %4, %2 offset:1024\n\t"
               "global_load_lds_dwordx4 %5, %2 offset:2048\n\t"
               "global_load_lds_dwordx4 %6, %2 offset:3072\n\t"
               "s_mov_b32 m0, %0"
               : "=&s"(keep) : "s"(lds_dst), "s"(sbase), "v"(v0), "v"(v1), "v"(v2), "v"(v3) : "memory");
}
#define BAR() asm volatile("s_waitcnt lgkmcnt(0)\n\ts_barrier" ::: "memory")
#define VMCNT_(n) asm volatile("s_waitcnt vmcnt(" #n ")" ::: "memory")
#define VMCNT(n) VMCNT_(n)

__device__ __forceinline__ float xg_max(float x) {
  auto s = __builtin_amdgcn_permlane16_swap(__float_as_uint(x), __float_as_uint(x), false, false);
  x = fmaxf(__uint_as_float(s[0]), __uint_as_float(s[1]));
  auto t = __builtin_amdgcn_permlane32_swap(__float_as_uint(x), __float_as_uint(x), false, false);
  return fmaxf(__uint_as_float(t[0]), __uint_as_float(t[1]));
}
__device__ __forceinline__ float xg_sum(float x) {
  auto s = __builtin_amdgcn_permlane16_swap(__float_as_uint(x), __float_as_uint(x), false, false);
  x = __uint_as_float(s[0]) + __uint_as_float(s[1]);
  auto t = __builtin_amdgcn_permlane32_swap(__float_as_uint(x), __float_as_uint(x), false, false);
  return __uint_as_float(t[0]) + __uint_as_float(t[1]);
}
__device__ __forceinline__ float ror8(float x) { return __builtin_bit_cast(float, __builtin_amdgcn_mov_dpp(__builtin_bit_cast(int, x), 0x128, 0xf, 0xf, true)); }
__device__ __forceinline__ unsigned pay(unsigned u) { return u == EMPTY ? 0x7FC07FC0u : u; }
__device__ __forceinline__ unsigned pay(float v) { return pay(__float_as_uint(v)); }
__device__ __forceinline__ void st128(__amdgpu_buffer_rsrc_t rs, unsigned off, u32x4 v) { __builtin_amdgcn_raw_buffer_store_b128(v, rs, off, 0, 16); }
__device__ __forceinline__ u32x4 ld128(__amdgpu_buffer_rsrc_t rs, unsigned off) { asm volatile("" ::: "memory"); return __builtin_amdgcn_raw_buffer_load_b128(rs, off, 0, 16); }
__device__ __forceinline__ bool full(u32x4 v) { return v[0] != EMPTY && v[1] != EMPTY && v[2] != EMPTY && v[3] != EMPTY; }

struct Ptrs {
  const uint16_t* x[2]; const uint16_t* k[2]; const uint16_t* v[2]; const uint16_t* wq[2]; const uint16_t* wo[2];
  uint16_t* out; unsigned char* ws;
};

struct KFrag { bf16x8 f[8]; };
struct VFrag { bf16x4 lo[8], hi[8]; };

__device__ __forceinline__ void readK(KFrag& k, const unsigned (&ak)[4]) {
  asm volatile("ds_read_b128 %0, %8\n\tds_read_b128 %1, %9\n\tds_read_b128 %2, %10\n\tds_read_b128 %3, %11\n\t"
               "ds_read_b128 %4, %8 offset:4096\n\tds_read_b128 %5, %9 offset:4096\n\tds_read_b128 %6, %10 offset:4096\n\tds_read_b128 %7, %11 offset:4096\n\t"
               "s_waitcnt lgkmcnt(0)"
               : "=&v"(k.f[0]), "=&v"(k.f[1]), "=&v"(k.f[2]), "=&v"(k.f[3]), "=&v"(k.f[4]), "=&v"(k.f[5]), "=&v"(k.f[6]), "=&v"(k.f[7])
               : "v"(ak[0]), "v"(ak[1]), "v"(ak[2]), "v"(ak[3]) : "memory");
}
__device__ __forceinline__ void readV(VFrag& v, unsigned av0) {
#pragma unroll
  for (int hb = 0; hb < 2; ++hb) {
    const unsigned a0 = av0 ^ ((4 * hb + 0) << 5), a1 = av0 ^ ((4 * hb + 1) << 5), a2 = av0 ^ ((4 * hb + 2) << 5), a3 = av0 ^ ((4 * hb + 3) << 5);
    asm volatile("ds_read_b64_tr_b16 %0, %8\n\tds_read_b64_tr_b16 %1, %9\n\tds_read_b64_tr_b16 %2, %10\n\tds_read_b64_tr_b16 %3, %11\n\t"
                 "ds_read_b64_tr_b16 %4, %8 offset:4096\n\tds_read_b64_tr_b16 %5, %9 offset:4096\n\tds_read_b64_tr_b16 %6, %10 offset:4096\n\tds_read_b64_tr_b16 %7, %11 offset:4096\n\t"
                 "s_waitcnt lgkmcnt(0)"
                 : "=&v"(v.lo[4 * hb + 0]), "=&v"(v.lo[4 * hb + 1]), "=&v"(v.lo[4 * hb + 2]), "=&v"(v.lo[4 * hb + 3]),
                   "=&v"(v.hi[4 * hb + 0]), "=&v"(v.hi[4 * hb + 1]), "=&v"(v.hi[4 * hb + 2]), "=&v"(v.hi[4 * hb + 3])
                 : "v"(a0), "v"(a1), "v"(a2), "v"(a3) : "memory");
  }
}
__device__ __forceinline__ void tile_qk(const KFrag& k, const bf16x8 (&qf)[4], float& m, float& l, f32x4 (&o)[8], bf16x8& pb) {
  f32x4 s0 = {0, 0, 0, 0}, s1 = {0, 0, 0, 0};
#pragma unroll
  for (int s = 0; s < 4; ++s) { s0 = MFMA16(k.f[s], qf[s], s0); s1 = MFMA16(k.f[4 + s], qf[s], s1); }
  float mx = fmaxf(fmaxf(fmaxf(s0[0], s0[1]), fmaxf(s0[2], s0[3])), fmaxf(fmaxf(s1[0], s1[1]), fmaxf(s1[2], s1[3])));
  mx = xg_max(mx);
  const float mn = fmaxf(m, mx);
  const float ms = (mn == -INFINITY) ? 0.f : mn;
  const float alpha = __builtin_amdgcn_exp2f((m - ms) * SCALE_LOG2E);
  const float mc = ms * SCALE_LOG2E;
  float p[8];
#pragma unroll
  for (int jj = 0; jj < 4; ++jj) { p[jj] = __builtin_amdgcn_exp2f(fmaf(s0[jj], SCALE_LOG2E, -mc)); p[4 + jj] = __builtin_amdgcn_exp2f(fmaf(s1[jj], SCALE_LOG2E, -mc)); }
  l = l * alpha + (((p[0] + p[1]) + (p[2] + p[3])) + ((p[4] + p[5]) + (p[6] + p[7])));
  m = mn;
  u32x4 pk = {cvt_pk_bf16(p[0], p[1]), cvt_pk_bf16(p[2], p[3]), cvt_pk_bf16(p[4], p[5]), cvt_pk_bf16(p[6], p[7])};
  pb = __builtin_bit_cast(bf16x8, pk);
#pragma unroll
  for (int blk = 0; blk < 8; ++blk) o[blk] *= alpha;
}
__device__ __forceinline__ void tile_pv(const VFrag& v, const bf16x8& pb, f32x4 (&o)[8]) {
#pragma unroll
  for (int blk = 0; blk < 8; ++blk) o[blk] = MFMA16(__builtin_shufflevector(v.lo[blk], v.hi[blk], 0, 1, 2, 3, 4, 5, 6, 7), pb, o[blk]);
}
__device__ __forceinline__ void tile_compute(const KFrag& k, const VFrag& v, const bf16x8 (&qf)[4], float& m, float& l, f32x4 (&o)[8]) {
  bf16x8 pb;
  tile_qk(k, qf, m, l, o, pb);
  tile_pv(v, pb, o);
}

__global__ void __launch_bounds__(NTHR, 2) k_fused(Ptrs P) {
  extern __shared__ __attribute__((aligned(16))) unsigned char smem[];
  const int tid = threadIdx.x, lane = tid & 63, wave = __builtin_amdgcn_readfirstlane(tid >> 6);
  const int bid = blockIdx.x;
  const int br = bid >> 7, seq = (bid >> 1) & 63, half = bid & 1, b = seq >> 3, kvh = seq & 7;
  const int j = b * 2 + half;
  const int grp = br * 128 + kvh * 16, wg = grp + j;
  const int g = lane >> 4, r16 = lane & 15, q4 = r16 >> 2, p4 = lane & 3;
  const unsigned lds0 = (unsigned)(size_t)(LAS unsigned char*)smem;
  volatile LAS unsigned* const ctl = (volatile LAS unsigned*)(smem + LDS_CTL);
  if (wave != 0) {
    const int sw = wave - 1;
    const unsigned slotK = lds0 + sw * 16384, slotV = slotK + 8192;
    const size_t tb = (size_t)seq * SK * 256 + (size_t)half * (TILES + BIAS) * 8192;
    constexpr size_t TS = 8192;
    const int ntiles = half ? TILES - BIAS : TILES + BIAS;
    const unsigned char* kbase = (const unsigned char*)P.k[br] + tb;
    const unsigned char* vbase = (const unsigned char*)P.v[br] + tb;
    unsigned vk[4], vv[2];
#pragma unroll
    for (int p = 0; p < 4; ++p) vk[p] = (unsigned)(g * 256 + ((r16 ^ ((4 * p + g) & 15)) * 16));
#pragma unroll
    for (int p = 0; p < 2; ++p) vv[p] = (unsigned)(g * 256 + ((r16 ^ (((4 * p + g) & 7) << 1)) * 16));
    auto issueK = [&](int t) { const unsigned char* a = kbase + (size_t)t * TS; dma4(slotK, a, vk[0], vk[1], vk[2], vk[3]); dma4(slotK + 4096, a + 4096, vk[0], vk[1], vk[2], vk[3]); };
    auto issueV = [&](int t) { const unsigned char* a = vbase + (size_t)t * TS; dma4(slotV, a, vv[0], vv[1], vv[0], vv[1]); dma4(slotV + 4096, a + 4096, vv[0], vv[1], vv[0], vv[1]); };
    BAR();
    issueK(sw); issueV(sw);
    unsigned ak[4], av0;
#pragma unroll
    for (int s = 0; s < 4; ++s) ak[s] = slotK + r16 * 256 + (((4 * s + g) ^ r16) * 16);
    av0 = slotV + (4 * g + q4) * 256 + (((4 * g + q4) & 7) << 5) + p4 * 8;
    auto landed = [&]() { if (lane == 0) __hip_atomic_fetch_add((LAS unsigned*)(smem + LDS_CTL) + 9, 1u, __ATOMIC_RELAXED, __HIP_MEMORY_SCOPE_WORKGROUP); };
    auto is_due = [&](int n) {
      const int i = __builtin_amdgcn_readfirstlane(ctl[10]) * 4 + 12;
      const unsigned tb = __builtin_amdgcn_readfirstlane(ctl[i]), tau = __builtin_amdgcn_readfirstlane(ctl[i + 2]);
      const int d = n * 256 - (int)__builtin_amdgcn_readfirstlane(ctl[i + 1]);
      if (!PACE || d <= 0) return true;
      return (unsigned)__builtin_amdgcn_s_memrealtime() - tb >= (unsigned)(((unsigned long long)(unsigned)d * tau) >> 16);
    };
    KFrag kA, kB; VFrag vA, vB;
    VMCNT(8); readK(kA, ak); landed(); issueK(sw + SW);
    VMCNT(8); readV(vA, av0); landed(); issueV(sw + SW);
    VMCNT(8); readK(kB, ak); landed(); issueK(sw + 2 * SW);
    VMCNT(8); readV(vB, av0); landed(); issueV(sw + 2 * SW);
    KFrag kC; VFrag vC;
    VMCNT(8); readK(kC, ak); landed(); issueK(sw + 3 * SW);
    VMCNT(8); readV(vC, av0); landed(); issueV(sw + 3 * SW);
    while (!__builtin_amdgcn_readfirstlane(ctl[0])) __builtin_amdgcn_s_sleep(1);
    asm volatile("" ::: "memory");
    bf16x8 qf[4];
    {
      const uint16_t* qp = (const uint16_t*)(smem + LDS_Q) + (r16 & 3) * DH + 8 * g;
#pragma unroll
      for (int s = 0; s < 4; ++s) { bf16x8 t = *(const bf16x8*)(qp + 32 * s); if (r16 >= 4) t = bf16x8{0, 0, 0, 0, 0, 0, 0, 0}; qf[s] = t; }
    }
    float m = -INFINITY, l = 0.f;
    f32x4 o[8];
#pragma unroll
    for (int i = 0; i < 8; ++i) o[i] = f32x4{0, 0, 0, 0};
    tile_compute(kA, vA, qf, m, l, o); tile_compute(kB, vB, qf, m, l, o); tile_compute(kC, vC, qf, m, l, o);
    for (int t = sw + 3 * SW; t < ntiles; t += SW) {
      KFrag kc; VFrag vc;
      if (sw == 4 && t + LEAD * SW >= ntiles && lane == 0) ctl[8] = 1u;
      const bool more = t + SW < ntiles;
      VMCNT(8);
      readK(kc, ak); landed();
      const bool early = more && is_due(t + SW);
      if (early) issueK(t + SW);
      bf16x8 pb;
      tile_qk(kc, qf, m, l, o, pb);
      asm volatile("" : "+v"(pb), "+v"(o[0]), "+v"(o[1]), "+v"(o[2]), "+v"(o[3]), "+v"(o[4]), "+v"(o[5]), "+v"(o[6]), "+v"(o[7]));
      if (early) VMCNT(8); else VMCNT(0);
      readV(vc, av0); landed();
      if (early) issueV(t + SW);
      tile_pv(vc, pb, o);
      if (more && !early) {
        while (!is_due(t + SW)) __builtin_amdgcn_s_sleep(1);
        issueK(t + SW); issueV(t + SW);
      }
    }
    l = xg_sum(l);
    float* mo = (float*)(smem + sw * 16384);
    if (r16 < 4) {
#pragma unroll
      for (int blk = 0; blk < 8; ++blk) *(f32x4*)(mo + r16 * 128 + 16 * blk + 4 * g) = o[blk];
      if (g == 0) { mo[512 + r16 * 2] = m; mo[512 + r16 * 2 + 1] = l; }
    }
    asm volatile("s_waitcnt lgkmcnt(0)" ::: "memory");
    if (lane == 0) ctl[1 + sw] = 1u;
    return;
  }
  __builtin_amdgcn_s_setprio(3);
  const unsigned tstart = (unsigned)__builtin_amdgcn_s_memrealtime();
  const int ntiles = half ? TILES - BIAS : TILES + BIAS;
  const __amdgpu_buffer_rsrc_t rs = __builtin_amdgcn_make_buffer_rsrc(P.ws, 0, WS_END, 0x00020000);
  const u32x4 EMPTY4 = {EMPTY, EMPTY, EMPTY, EMPTY};
  const unsigned qin = WS_QIN + wg * IN_WG + lane * 16;
  const int th = j >> 2, cbk = j & 3, hd = kvh * 4 + th;
  const int js = half * 8 + b;
  st128(rs, qin, EMPTY4);
  if (lane < 24) st128(rs, qin + 1024, EMPTY4);
  for (int i = lane; i < (int)(OA_USED / 16); i += 64) st128(rs, WS_OA + wg * OA_WG + i * 16, EMPTY4);
#pragma unroll
  for (int k = 0; k < 4; ++k) st128(rs, WS_OB + wg * 4096 + (lane + 64 * k) * 16, EMPTY4);
  {
    unsigned vx[4];
#pragma unroll
    for (int p = 0; p < 4; ++p) vx[p] = (unsigned)((lane >> 3) * (DM * 2) + (lane & 7) * 16 + 3072 - 896 * p);
    const unsigned char* xb = (const unsigned char*)P.x[br] - 3072;
    dma4d(lds0 + LDS_X, xb, vx[0], vx[1], vx[2], vx[3]); dma4d(lds0 + LDS_X + 4096, xb + 512, vx[0], vx[1], vx[2], vx[3]);
    const unsigned char* sb = (const unsigned char*)P.wq[br] + (kvh * 512 + 32 * js) * 2;
    unsigned vo[4];
#pragma unroll
    for (int p = 0; p < 4; ++p) vo[p] = (unsigned)((16 * p + (lane >> 2)) * 8192 + (lane & 3) * 16 - 1024 * p);
#pragma unroll
    for (int c = 0; c < 8; ++c) dma4(lds0 + LDS_W + c * 4096, sb + (size_t)c * 64 * 8192, vo[0], vo[1], vo[2], vo[3]);
  }
  if (lane < 16) ctl[lane] = lane == 12 ? tstart : lane == 13 ? 3u * 256u : lane == 14 ? (unsigned)(PINIT * 256 / ntiles) : 0u;
  BAR();
  u32x4 pk;
  {
    f32x4 acc0 = {0, 0, 0, 0}, acc1 = {0, 0, 0, 0};
#pragma unroll
    for (int c = 0; c < 8; ++c) {
      const unsigned a0 = lds0 + LDS_W + c * 4096 + (8 * g + q4) * 64 + p4 * 16;
      const unsigned ax = lds0 + LDS_X + c * 1024 + (r16 & 7) * 128 + g * 16;
      bf16x4 w[8]; bf16x8 xb0, xb1;
      asm volatile("s_waitcnt vmcnt(%12)\n\t"
                   "ds_read_b64_tr_b16 %0, %10\n\tds_read_b64_tr_b16 %1, %10 offset:256\n\t"
                   "ds_read_b64_tr_b16 %2, %10 offset:8\n\tds_read_b64_tr_b16 %3, %10 offset:264\n\t"
                   "ds_read_b64_tr_b16 %4, %10 offset:2048\n\tds_read_b64_tr_b16 %5, %10 offset:2304\n\t"
                   "ds_read_b64_tr_b16 %6, %10 offset:2056\n\tds_read_b64_tr_b16 %7, %10 offset:2312\n\t"
                   "ds_read_b128 %8, %11\n\tds_read_b128 %9, %11 offset:64\n\t"
                   "s_waitcnt lgkmcnt(0)"
                   : "=&v"(w[0]), "=&v"(w[1]), "=&v"(w[2]), "=&v"(w[3]), "=&v"(w[4]), "=&v"(w[5]), "=&v"(w[6]), "=&v"(w[7]), "=&v"(xb0), "=&v"(xb1)
                   : "v"(a0), "v"(ax), "i"((7 - c) * 4) : "memory");
      acc0 = MFMA16(__builtin_shufflevector(w[0], w[1], 0, 1, 2, 3, 4, 5, 6, 7), xb0, acc0);
      acc1 = MFMA16(__builtin_shufflevector(w[2], w[3], 0, 1, 2, 3, 4, 5, 6, 7), xb0, acc1);
      acc0 = MFMA16(__builtin_shufflevector(w[4], w[5], 0, 1, 2, 3, 4, 5, 6, 7), xb1, acc0);
      acc1 = MFMA16(__builtin_shufflevector(w[6], w[7], 0, 1, 2, 3, 4, 5, 6, 7), xb1, acc1);
    }
    pk[0] = pay(cvt_pk_bf16(acc0[0], acc0[1])); pk[1] = pay(cvt_pk_bf16(acc0[2], acc0[3])); pk[2] = pay(cvt_pk_bf16(acc1[0], acc1[1])); pk[3] = pay(cvt_pk_bf16(acc1[2], acc1[3]));
  }
  const unsigned qdst = WS_QIN + (grp + (r16 & 7) * 2 + (r16 >> 3)) * IN_WG + js * 64 + g * 16;
  const unsigned hdst = WS_QIN + ((br << 7) + (lane & 31) * 4 + cbk) * IN_WG + 1024 + hd * 4;
  auto send_q = [&]() { st128(rs, qdst, pk); };
  auto send_h = [&]() { if (lane < 32) __builtin_amdgcn_raw_buffer_store_b32(1u, rs, hdst, 0, 16); };
  send_q(); send_h();
  for (unsigned spins = 0;; ++spins) {
    const u32x4 x0 = ld128(rs, qin);
    if (__all(full(x0)) || spins > SPIN_CAP) {
      ((u32x4*)(smem + LDS_Q))[lane] = x0;
      asm volatile("s_waitcnt lgkmcnt(0)" ::: "memory");
      if (lane == 0) ctl[0] = 1u;
      break;
    }
  }
  send_q();
  if (PACE) {
    auto now = [&]() { return (unsigned)__builtin_amdgcn_s_memrealtime() - tstart; };
    auto lwg = [&](int bb) { return (bb >> 7) * 128 + ((bb >> 1) & 7) * 16 + ((bb >> 4) & 7) * 2 + (bb & 1); };
    while (now() < PT0) __builtin_amdgcn_s_sleep(4);
    const unsigned ta = now(), p0 = __builtin_amdgcn_readfirstlane(ctl[9]);
    const bool ontime = ta + 600 < PT1;
    while (now() < PT1) __builtin_amdgcn_s_sleep(4);
    const unsigned p1 = __builtin_amdgcn_readfirstlane(ctl[9]);
    const unsigned dp16 = ontime ? (p1 - p0) * 16u * (PT1 - PT0) / (PT1 - ta) : 0u;
    typedef __attribute__((ext_vector_type(2))) unsigned u32x2;
    if (ontime && lane < 32) __builtin_amdgcn_raw_buffer_store_b64(u32x2{p1 | (dp16 << 12), pay(tstart)}, rs, WS_QIN + lwg((bid & ~31) + lane) * IN_WG + 1152 + (bid & 31) * 8, 0, 16);
    u32x2 pv = {EMPTY, EMPTY};
    bool all = false;
    for (int spins = 0; spins < 12 && !all; ++spins) {
      asm volatile("" ::: "memory");
      pv = __builtin_amdgcn_raw_buffer_load_b64(rs, WS_QIN + wg * IN_WG + 1152 + (lane & 31) * 8, 0, 16);
      all = __all(pv[0] != EMPTY && pv[1] != EMPTY);
      if (!all) __builtin_amdgcn_s_sleep(8);
    }
    if (all) {
      float s1 = (float)(pv[0] & 0xfffu), sd = (float)(pv[0] >> 12), so = (float)(int)(pv[1] - tstart);
#pragma unroll
      for (int mk = 16; mk >= 1; mk >>= 1) { s1 += __shfl_xor(s1, mk); sd += __shfl_xor(sd, mk); so += __shfl_xor(so, mk); }
      const float rate = fmaxf(sd * (1.f / 1024.f) / (float)(PT1 - PT0), (float)TILES / PSLOW);
      const float tend = (float)PT1 + so * (1.f / 32.f) + ((float)TILES - s1 * (1.f / 64.f)) / (rate * (1.f - PEPS));
      const unsigned tn = now();
      const float pn = (float)__builtin_amdgcn_readfirstlane(ctl[9]) * 0.5f;
      const float tau = fminf(fmaxf((tend - (float)tn) / fmaxf((float)ntiles - pn, 1.f), 0.f), 100.f);
      if (lane == 0) { ctl[16] = tstart + tn; ctl[17] = (unsigned)((pn + PAHEAD) * 256.f); ctl[18] = (unsigned)(tau * 256.f); }
      asm volatile("s_waitcnt lgkmcnt(0)" ::: "memory");
      if (lane == 0) ctl[10] = 1u;
    }
  }
  for (unsigned spins = 0;; ++spins) {
    const u32x4 h0 = ld128(rs, WS_QIN + wg * IN_WG + 1024 + (lane & 7) * 16);
    if (__all(full(h0)) || spins > SPIN_CAP) break;
    __builtin_amdgcn_s_sleep(16);
  }
  send_h();
  bf16x4 wlo[8][4], whi[8][4];
  {
    float M = -INFINITY, L = 0.f; f32x4 a0 = {0, 0, 0, 0}, a1 = {0, 0, 0, 0};
    auto merge = [&](int w) {
      const float* d0 = (const float*)(smem + w * 16384);
      const f32x2 t = *(const f32x2*)(d0 + 512 + g * 2);
      const f32x4 x0 = *(const f32x4*)(d0 + g * 128 + r16 * 8), x1 = *(const f32x4*)(d0 + g * 128 + r16 * 8 + 4);
      const float Mn = fmaxf(M, t[0]), Ms = (Mn == -INFINITY) ? 0.f : Mn;
      const float so = __builtin_amdgcn_exp2f((M - Ms) * SCALE_LOG2E), sn = __builtin_amdgcn_exp2f((t[0] - Ms) * SCALE_LOG2E);
      L = L * so + t[1] * sn; a0 = a0 * so + x0 * sn; a1 = a1 * so + x1 * sn; M = Mn;
    };
    while (!__builtin_amdgcn_readfirstlane(ctl[8])) __builtin_amdgcn_s_sleep(8);
    {
      const unsigned char* sb = (const unsigned char*)P.wo[br] + (size_t)(hd * 128) * 1024 + cbk * 256;
      unsigned vo[4];
#pragma unroll
      for (int p = 0; p < 4; ++p) vo[p] = (unsigned)((4 * p + g) * 1024 + ((r16 ^ (((4 * p + g) & 7) << 1)) * 16) - 1024 * p);
#pragma unroll
      for (int c = 0; c < 8; ++c) dma4(lds0 + LDS_W + c * 4096, sb + c * 16384, vo[0], vo[1], vo[2], vo[3]);
    }
    {
      VMCNT(0);
      const unsigned b0 = lds0 + LDS_W + (8 * g + q4) * 256 + p4 * 8, b1 = b0 + 4 * 256;
#pragma unroll
      for (int cb8 = 0; cb8 < 8; ++cb8) {
        const unsigned a0_ = b0 + ((cb8 ^ q4) << 5), a1_ = b1 + ((cb8 ^ (q4 + 4)) << 5);
        asm volatile("ds_read_b64_tr_b16 %0, %8\n\tds_read_b64_tr_b16 %1, %8 offset:8192\n\tds_read_b64_tr_b16 %2, %8 offset:16384\n\tds_read_b64_tr_b16 %3, %8 offset:24576\n\t"
                     "ds_read_b64_tr_b16 %4, %9\n\tds_read_b64_tr_b16 %5, %9 offset:8192\n\tds_read_b64_tr_b16 %6, %9 offset:16384\n\tds_read_b64_tr_b16 %7, %9 offset:24576\n\t"
                     "s_waitcnt lgkmcnt(0)"
                     : "=&v"(wlo[cb8][0]), "=&v"(wlo[cb8][1]), "=&v"(wlo[cb8][2]), "=&v"(wlo[cb8][3]), "=&v"(whi[cb8][0]), "=&v"(whi[cb8][1]), "=&v"(whi[cb8][2]), "=&v"(whi[cb8][3])
                     : "v"(a0_), "v"(a1_) : "memory");
      }
    }
    while (__builtin_popcountll(__ballot(lane >= 1 && lane <= SW && ctl[lane & 15] != 0u)) != SW) __builtin_amdgcn_s_sleep(1);
    asm volatile("" ::: "memory");
#pragma unroll
    for (int w = 0; w < SW; ++w) merge(w);
    const unsigned oa = WS_OA + wg * OA_WG + g * (OA_H * 4);
    st128(rs, oa + r16 * 32, u32x4{pay(a0[0]), pay(a0[1]), pay(a0[2]), pay(a0[3])});
    st128(rs, oa + r16 * 32 + 16, u32x4{pay(a1[0]), pay(a1[1]), pay(a1[2]), pay(a1[3])});
    if (r16 == 0) st128(rs, oa + 512, u32x4{pay(M), pay(L), 0u, 0u});
  }
  bf16x8 of[4];
  {
    const unsigned rec = WS_OA + (grp + (r16 & 7) * 2 + (r16 >> 3)) * OA_WG + th * (OA_H * 4);
    u32x4 ml = EMPTY4, x[8];
    for (unsigned spins = 0;; ++spins) {
      if (g == 0 && !full(ml)) ml = ld128(rs, rec + 512);
      if (__builtin_popcountll(__ballot(g == 0 && !full(ml))) <= MISS || spins > SPIN_CAP) break;
      __builtin_amdgcn_s_sleep(8);
    }
    bool have = false;
    for (unsigned spins = 0;; ++spins) {
      if (!have) {
        ml = ld128(rs, rec + 512);
#pragma unroll
        for (int s = 0; s < 4; ++s) { x[2 * s] = ld128(rs, rec + s * 128 + g * 32); x[2 * s + 1] = ld128(rs, rec + s * 128 + g * 32 + 16); }
        bool ok = full(ml);
#pragma unroll
        for (int k = 0; k < 8; ++k) ok = ok && full(x[k]);
        have = ok;
      }
      if (__all(have) || spins > SPIN_CAP) break;
      __builtin_amdgcn_s_sleep(POLL_SLEEP);
    }
    const float mc = __uint_as_float(ml[0]), lc = __uint_as_float(ml[1]);
    const float M = fmaxf(mc, ror8(mc));
    const float wt = __builtin_amdgcn_exp2f((mc - M) * SCALE_LOG2E);
    float L = wt * lc; L += ror8(L);
    const float sc = wt / L;
#pragma unroll
    for (int s = 0; s < 4; ++s) {
      float v[8];
#pragma unroll
      for (int e = 0; e < 8; ++e) { float t = sc * __uint_as_float(x[2 * s + (e >> 2)][e & 3]); t += ror8(t); v[e] = t; }
      const u32x4 pk4 = {cvt_pk_bf16(v[0], v[1]), cvt_pk_bf16(v[2], v[3]), cvt_pk_bf16(v[4], v[5]), cvt_pk_bf16(v[6], v[7])};
      of[s] = __builtin_bit_cast(bf16x8, pk4);
    }
  }
  {
    f32x4 acc[8];
#pragma unroll
    for (int cb8 = 0; cb8 < 8; ++cb8) {
      acc[cb8] = f32x4{0, 0, 0, 0};
#pragma unroll
      for (int s = 0; s < 4; ++s) acc[cb8] = MFMA16(__builtin_shufflevector(wlo[cb8][s], whi[cb8][s], 0, 1, 2, 3, 4, 5, 6, 7), of[s], acc[cb8]);
    }
    const unsigned ob = WS_OB + wg * 4096 + (r16 & 7) * 512 + ((r16 >> 3) * 64 + 4 * g) * 4;
#pragma unroll
    for (int k = 0; k < 4; ++k) {
      const f32x4 lo4 = acc[k], hi4 = acc[4 + k];
      u32x4 e;
#pragma unroll
      for (int i = 0; i < 4; ++i) { float a_ = lo4[i], b_ = hi4[i]; asm volatile("" : "+v"(a_), "+v"(b_)); e[i] = pay(r16 >= 8 ? b_ : a_); }
      st128(rs, ob + k * 64, e);
    }
  }
  {
    const int fb = hd >> 2, fc = (hd & 3) * 32;
    const unsigned src = WS_OB + ((br << 7) + (lane >> 3) * 4 + cbk) * 4096 + (fb * 128 + fc + (lane & 7) * 4) * 4;
    u32x4 y[4];
    bool have = false;
    for (unsigned spins = 0;; ++spins) {
      if (!have) {
#pragma unroll
        for (int k = 0; k < 4; ++k) y[k] = ld128(rs, src + k * (32 * 4096));
        have = full(y[0]) && full(y[1]) && full(y[2]) && full(y[3]);
      }
      if (__all(have) || spins > SPIN_CAP) break;
      __builtin_amdgcn_s_sleep(POLL_SLEEP);
    }
    float sv[4];
#pragma unroll
    for (int e = 0; e < 4; ++e) {
      float t = (__uint_as_float(y[0][e]) + __uint_as_float(y[1][e])) + (__uint_as_float(y[2][e]) + __uint_as_float(y[3][e]));
      t += ror8(t);
      sv[e] = xg_sum(t);
    }
    if (lane < 8) *(uint2*)(P.out + fb * 1024 + br * 512 + cbk * 128 + fc + lane * 4) = make_uint2(cvt_pk_bf16(sv[0], sv[1]), cvt_pk_bf16(sv[2], sv[3]));
  }
}

extern "C" void kernel_launch(void* const* d_in, const int* in_sizes, int n_in, void* d_out, int out_size, void* d_ws, size_t ws_size, hipStream_t stream) {
  Ptrs P;
  for (int i = 0; i < 2; ++i) {
    P.x[i] = (const uint16_t*)d_in[5 * i + 0]; P.k[i] = (const uint16_t*)d_in[5 * i + 1]; P.v[i] = (const uint16_t*)d_in[5 * i + 2];
    P.wq[i] = (const uint16_t*)d_in[5 * i + 3]; P.wo[i] = (const uint16_t*)d_in[5 * i + 4];
  }
  P.out = (uint16_t*)d_out; P.ws = (unsigned char*)d_ws;
  (void)hipFuncSetAttribute((const void*)k_fused, hipFuncAttributeMaxDynamicSharedMemorySize, LDS_TOTAL);
  k_fused<<<NWG, NTHR, LDS_TOTAL, stream>>>(P);
}
```
